```python
import torch

def _softmax(x, axis=-1):
    return torch.softmax(x, dim=axis)
def _gelu(x):
    return torch.nn.functional.gelu(x, approximate="tanh")
def _silu(x):
    return torch.nn.functional.silu(x)
def _sigmoid(x):
    return torch.sigmoid(x)
def _layernorm(x, axis=-1, eps=1e-5):
    m = x.mean(dim=axis, keepdim=True)
    v = x.var(dim=axis, keepdim=True, unbiased=False)
    return (x - m) / torch.sqrt(v + eps)
def _scatter_add(tgt, idx, src, axis=0):
    return tgt.clone().index_add_(axis, idx, src)
def _segment_sum(src, seg, n):
    out = torch.zeros((n,) + src.shape[1:], dtype=src.dtype)
    return out.index_add_(0, seg, src)
def _pad(x, pw):
    flat = tuple(v for pair in reversed(pw) for v in pair)
    return torch.nn.functional.pad(x, flat)

kernel_name = 'recurrent_scan__linear_attn_block__glu_gated_geglu__prompt'

def setup_inputs():

    _w = (lambda a: (lambda t: t.to(torch.bfloat16) if t.is_floating_point() else t)(torch.as_tensor(a)))
    def _A_setup():
        B, S, D = 1, 2048, 640
        return {
            "x": _w((lambda *s: torch.randn(*s, dtype=torch.bfloat16) * 0.05)(B, S, D)),
            "wqkv": _w((lambda *s: torch.randn(*s, dtype=torch.bfloat16) * 0.8)(D, 3 * D)),
            "wo": _w((lambda *s: torch.randn(*s, dtype=torch.bfloat16) * 0.8)(D, D)),
        }
    _A_in = _A_setup()

    def _B_setup():
        B, S, Dm, Df = 1, 2048, 640, 1536
        return {
            "x": _w((lambda *s: torch.randn(*s, dtype=torch.bfloat16) * 0.05)(B, S, Dm)),
            "w_gate": _w((lambda *s: torch.randn(*s, dtype=torch.bfloat16) * 0.2)(Dm, Df) * (Dm ** -0.5)),
            "w_up": _w((lambda *s: torch.randn(*s, dtype=torch.bfloat16) * 0.2)(Dm, Df) * (Dm ** -0.5)),
            "w_down": _w((lambda *s: torch.randn(*s, dtype=torch.bfloat16) * 0.2)(Df, Dm) * (Df ** -0.5)),
        }
    _B_in = _B_setup()

    return {
        "A_x": _A_in["x"],
        "A_wqkv": _A_in["wqkv"],
        "A_wo": _A_in["wo"],
        "B_x": _B_in["x"],
        "B_w_gate": _B_in["w_gate"],
        "B_w_up": _B_in["w_up"],
        "B_w_down": _B_in["w_down"],
    }

def reference(A_x, A_wqkv, A_wo, B_x, B_w_gate, B_w_up, B_w_down):
    def _A_ref(x, wqkv, wo):
        B, S, D = x.shape
        H = 32; Dh = D // H
        qkv = (lambda x, *d: x.permute(*d))((x @ wqkv).reshape(B, S, 3, H, Dh), 2, 0, 3, 1, 4)
        q, k, v = qkv[0], qkv[1], qkv[2]
        phi = lambda t: torch.where(t > 0, t + 1, torch.exp(t)) + 1e-6
        pq, pk = phi(q), phi(k)
        kv = torch.einsum("bhsd,bhse->bhde", pk, v)
        z = torch.einsum("bhsd->bhd", pk)
        num = torch.einsum("bhqd,bhde->bhqe", pq, kv)
        den = torch.einsum("bhqd,bhd->bhq", pq, z)[..., None] + 1e-6
        o = num / den
        return (lambda x, *d: x.permute(*d))(o, 0, 2, 1, 3).reshape(B, S, D) @ wo

    def _B_ref(x, w_gate, w_up, w_down):
        g = torch.einsum("bsd,df->bsf", x, w_gate)
        u = torch.einsum("bsd,df->bsf", x, w_up)
        h = _gelu(g) * u
        return torch.einsum("bsf,fd->bsd", h, w_down)

    x = A_x
    xn = _layernorm(x, axis=-1)
    s = _A_ref(xn, A_wqkv, A_wo)
    g = _silu(_B_ref(xn, B_w_gate, B_w_up, B_w_down))
    return x + s * g
```

```cpp
#include <hip/hip_runtime.h>
#include <hip/hip_bf16.h>
#include <stdint.h>

typedef __bf16 bf16x8 __attribute__((ext_vector_type(8)));
typedef __bf16 bf16x4 __attribute__((ext_vector_type(4)));
typedef __bf16 bf16x2 __attribute__((ext_vector_type(2)));
typedef float f32x4 __attribute__((ext_vector_type(4)));
typedef float f32x2 __attribute__((ext_vector_type(2)));
typedef unsigned v4u __attribute__((ext_vector_type(4)));
typedef unsigned v2u __attribute__((ext_vector_type(2)));
typedef int v4i __attribute__((ext_vector_type(4)));
#define LAS __attribute__((address_space(3)))
#define DEVI __device__ __forceinline__

constexpr int S_ = 2048, D_ = 640, DF_ = 1536;

constexpr unsigned WS_HB = 0;
constexpr unsigned WS_GO = 128;
constexpr unsigned WS_HF = 2048;
constexpr unsigned WS_QF = WS_HF + 156 * 64;
constexpr unsigned WS_SIDE = 16384;
constexpr unsigned WS_RP = 32768;
constexpr unsigned WS_CP = WS_RP + 10 * 2080 * 8 + 1536;
constexpr unsigned WS_KVP = WS_CP + 10 * 5120 * 4;
constexpr unsigned WS_ZP = WS_KVP + 32 * 8 * 1664;
constexpr unsigned WS_MF = WS_ZP + 32 * 16 * 256;
constexpr unsigned WS_PQ = WS_MF + 160 * 20 * 256;
constexpr unsigned WS_H = WS_PQ + 128 * 20 * 1024;
constexpr unsigned WS_END = WS_H + 128 * 48 * 1024;
constexpr unsigned POISON = 0xffffffffu;
constexpr unsigned SPIN_CAP = 1u << 20;
constexpr unsigned CODEPF = 90000;

DEVI v4i make_rsrc(const void* p, unsigned bytes) {
  uint64_t a = (uint64_t)p;
  v4i r;
  r.x = (int)(unsigned)a;
  r.y = (int)((unsigned)(a >> 32) & 0xffffu);
  r.z = (int)bytes;
  r.w = 0x00020000;
  return r;
}
DEVI void dma16(v4i rsrc, unsigned voff, unsigned soff, unsigned lds) {
  asm volatile("s_mov_b32 m0, %3\n\ts_nop 0\n\tbuffer_load_dwordx4 %0, %1, %2 offen lds" ::"v"(voff), "s"(rsrc), "s"(soff), "s"(lds) : "memory");
}
DEVI void dma16m(v4i rsrc, unsigned voff, unsigned soff, unsigned lds, int mask) {
  asm volatile("s_mov_b32 m0, %3\n\ts_mov_b32 exec_lo, %4\n\ts_mov_b32 exec_hi, %4\n\tbuffer_load_dwordx4 %0, %1, %2 offen lds\n\ts_mov_b64 exec, -1"
               ::"v"(voff), "s"(rsrc), "s"(soff), "s"(lds), "s"(mask) : "memory");
}
DEVI void ald16(v4u& dst, v4i rsrc, unsigned voff, unsigned soff) {
  asm volatile("s_nop 4\n\tbuffer_load_dwordx4 %0, %1, %2, %3 offen sc1" : "=v"(dst) : "v"(voff), "s"(rsrc), "s"(soff) : "memory");
}
DEVI void ald4(unsigned& dst, v4i rsrc, unsigned voff, unsigned soff) {
  asm volatile("s_nop 4\n\tbuffer_load_dword %0, %1, %2, %3 offen sc1" : "=v"(dst) : "v"(voff), "s"(rsrc), "s"(soff) : "memory");
}
DEVI void ast16(v4u v, v4i rsrc, unsigned voff, unsigned soff) {
  asm volatile("s_nop 4\n\tbuffer_store_dwordx4 %0, %1, %2, %3 offen sc1\n\ts_nop 1" ::"v"(v), "v"(voff), "s"(rsrc), "s"(soff) : "memory");
}
DEVI void ast8(v2u v, v4i rsrc, unsigned voff, unsigned soff) {
  asm volatile("s_nop 4\n\tbuffer_store_dwordx2 %0, %1, %2, %3 offen sc1\n\ts_nop 1" ::"v"(v), "v"(voff), "s"(rsrc), "s"(soff) : "memory");
}
DEVI void ast4(unsigned v, v4i rsrc, unsigned voff, unsigned soff) {
  asm volatile("s_nop 4\n\tbuffer_store_dword %0, %1, %2, %3 offen sc1\n\ts_nop 1" ::"v"(v), "v"(voff), "s"(rsrc), "s"(soff) : "memory");
}
#define EXM_ON(n) "s_mov_b32 exec_lo, %" #n "\n\ts_mov_b32 exec_hi, %" #n "\n\ts_nop 2\n\t"
#define EXM_OFF "\n\ts_mov_b64 exec, -1"
DEVI void ald16m(v4u& dst, v4i rsrc, unsigned voff, unsigned soff, int mask) {
  asm volatile(EXM_ON(4) "buffer_load_dwordx4 %0, %1, %2, %3 offen sc1" EXM_OFF : "+v"(dst) : "v"(voff), "s"(rsrc), "s"(soff), "s"(mask) : "memory");
}
DEVI void ald4m(unsigned& dst, v4i rsrc, unsigned voff, unsigned soff, int mask) {
  asm volatile(EXM_ON(4) "buffer_load_dword %0, %1, %2, %3 offen sc1" EXM_OFF : "+v"(dst) : "v"(voff), "s"(rsrc), "s"(soff), "s"(mask) : "memory");
}
DEVI void ast16m(v4u v, v4i rsrc, unsigned voff, unsigned soff, int mask) {
  asm volatile(EXM_ON(4) "buffer_store_dwordx4 %0, %1, %2, %3 offen sc1" EXM_OFF "\n\ts_nop 0" ::"v"(v), "v"(voff), "s"(rsrc), "s"(soff), "s"(mask) : "memory");
}
DEVI int negmask(int v) {
  int m;
  asm("s_ashr_i32 %0, %1, 31" : "=s"(m) : "s"(v) : "scc");
  return m;
}
template <int N> DEVI void wait_vm() { asm volatile("s_waitcnt vmcnt(%0)" ::"i"(N) : "memory"); }
DEVI void wait_lgkm0() { asm volatile("s_waitcnt lgkmcnt(0)" ::: "memory"); }
DEVI void bar() { __builtin_amdgcn_s_barrier(); }
DEVI void sched0() { __builtin_amdgcn_sched_barrier(0); }
DEVI void tie(v4u& a) { asm volatile("; tie %0" : "+v"(a)); }
DEVI void tie(unsigned& a) { asm volatile("; tie %0" : "+v"(a)); }

template <int V> struct IC { static constexpr int value = V; };
template <int I, int N, class F> DEVI void static_for(F&& f) {
  if constexpr (I < N) { f(IC<I>{}); static_for<I + 1, N>(f); }
}
DEVI unsigned sel(bool c, unsigned a, unsigned b) { asm volatile("" : "+v"(a)); return c ? a : b; }
DEVI float bflo(unsigned u) { return __uint_as_float(u << 16); }
DEVI float bfhi(unsigned u) { return __uint_as_float(u & 0xffff0000u); }
DEVI unsigned pack2(float a, float b) {
  f32x2 p = {a, b};
  bf16x2 q = __builtin_convertvector(p, bf16x2);
  return __builtin_bit_cast(unsigned, q);
}
DEVI unsigned nopoison(unsigned u) { return u < 0xfffffffeu ? u : 0xfffffffeu; }
DEVI unsigned pack2s(float a, float b) { return nopoison(pack2(a, b)); }
DEVI f32x4 exp2v(f32x4 a) {
  return f32x4{__builtin_amdgcn_exp2f(a[0]), __builtin_amdgcn_exp2f(a[1]), __builtin_amdgcn_exp2f(a[2]), __builtin_amdgcn_exp2f(a[3])};
}
DEVI f32x4 rcpv(f32x4 a) {
  return f32x4{__builtin_amdgcn_rcpf(a[0]), __builtin_amdgcn_rcpf(a[1]), __builtin_amdgcn_rcpf(a[2]), __builtin_amdgcn_rcpf(a[3])};
}
DEVI float exp2_le1(float x) { return __builtin_amdgcn_fmed3f(__builtin_amdgcn_exp2f(x), 0.f, 1.f); }
DEVI f32x4 phis(f32x4 a, f32x4 rs, f32x4 rl) {
  const f32x4 x = a * rl, l = a * rs + 1e-6f;
  const f32x4 e = {exp2_le1(x[0]), exp2_le1(x[1]), exp2_le1(x[2]), exp2_le1(x[3])};
  const f32x4 m = {__builtin_fmaxf(l[0], 1e-6f), __builtin_fmaxf(l[1], 1e-6f), __builtin_fmaxf(l[2], 1e-6f), __builtin_fmaxf(l[3], 1e-6f)};
  return e + m;
}
DEVI v2u pack4(f32x4 a) { return v2u{pack2(a[0], a[1]), pack2(a[2], a[3])}; }
DEVI bf16x8 tr_frag(const LAS char* p0, const LAS char* p1) {
  bf16x4 a = __builtin_amdgcn_ds_read_tr16_b64_v4bf16((LAS bf16x4*)p0);
  bf16x4 b = __builtin_amdgcn_ds_read_tr16_b64_v4bf16((LAS bf16x4*)p1);
  return __builtin_shufflevector(a, b, 0, 1, 2, 3, 4, 5, 6, 7);
}
DEVI f32x4 mfma16(bf16x8 a, bf16x8 b, f32x4 c) { return __builtin_amdgcn_mfma_f32_16x16x32_bf16(a, b, c, 0, 0, 0); }
DEVI f32x4 mfma16(v4u a, v4u b, f32x4 c) {
  return __builtin_amdgcn_mfma_f32_16x16x32_bf16(__builtin_bit_cast(bf16x8, a), __builtin_bit_cast(bf16x8, b), c, 0, 0, 0);
}
DEVI void xhalf(v4u& a, v4u& b) {
  unsigned p = a.z, q = b.x, u = a.w, v = b.y;
  asm("v_swap_b32 %0, %1\n\tv_swap_b32 %2, %3\n\ts_nop 1" : "+v"(p), "+v"(q), "+v"(u), "+v"(v));
  a.z = p; b.x = q; a.w = u; b.y = v;
}
DEVI unsigned umax4(v4u a) { return max(max(a.x, a.y), max(a.z, a.w)); }
DEVI float sum16(float v) {
  const unsigned u = __float_as_uint(v);
  auto r = __builtin_amdgcn_permlane16_swap(u, u, false, false);
  return __uint_as_float(r[0]) + __uint_as_float(r[1]);
}
DEVI float sum32(float v) {
  const unsigned u = __float_as_uint(v);
  auto r = __builtin_amdgcn_permlane32_swap(u, u, false, false);
  return __uint_as_float(r[0]) + __uint_as_float(r[1]);
}


struct Params {
  const __hip_bfloat16* x;
  const __hip_bfloat16* wqkv;
  const __hip_bfloat16* wo;
  const __hip_bfloat16* wg;
  const __hip_bfloat16* wu;
  const __hip_bfloat16* wd;
  __hip_bfloat16* out;
  char* ws;
};

constexpr int STAGE = 53248;
constexpr int T_COL = 3 * STAGE;
constexpr int T_CNT = STAGE;
constexpr int T_ROW = 152576, T_Z = T_ROW + 4096;
constexpr int SLOT = 4096;
__host__ __device__ constexpr int slot_grp(int q) { return q == 0 ? 54272 : (q == 1 ? 54272 + 12 * SLOT : 0); }
__host__ __device__ constexpr int slot_addr(int w, int j) { return slot_grp(j / 3) + (w * 3 + j % 3) * SLOT; }
static_assert(54272 + 12 * SLOT <= 2 * STAGE && 54272 + 24 * SLOT <= 3 * STAGE && 12 * SLOT <= STAGE, "slot map");
DEVI void lds_inc(LAS char* shm, int c) {
  const unsigned a = (unsigned)(uintptr_t)shm + T_CNT + c * 4;
  asm volatile("s_mov_b64 exec, 1\n\tds_add_u32 %0, %1\n\ts_mov_b64 exec, -1" ::"v"(a), "v"(1u) : "memory");
}
DEVI void lds_wait(LAS char* shm, int c, unsigned n) {
  const unsigned a = (unsigned)(uintptr_t)shm + T_CNT + c * 4;
  unsigned v, t, cnt;
  asm volatile("s_mov_b32 %2, %5\n"
               ".Lw%=a:\n\t"
               "ds_read_b32 %0, %3\n\ts_waitcnt lgkmcnt(0)\n\tv_readfirstlane_b32 %1, %0\n\ts_nop 0\n\t"
               "s_cmp_ge_u32 %1, %4\n\ts_cbranch_scc1 .Lw%=b\n\t"
               "s_sub_u32 %2, %2, 1\n\ts_cbranch_scc1 .Lw%=b\n\t"
               "s_sleep 1\n\ts_branch .Lw%=a\n"
               ".Lw%=b:"
               : "=&v"(v), "=&s"(t), "=&s"(cnt)
               : "v"(a), "s"(n), "i"(SPIN_CAP * 64)
               : "memory", "scc");
}
template <int SLEEP> DEVI void wait_ne(v4i rsrc, unsigned voff, unsigned bad) {
  unsigned v, cnt;
  asm volatile("s_mov_b32 %1, %5\n\ts_nop 3\n"
               ".Lp%=a:\n\t"
               "buffer_load_dword %0, %2, %3, 0 offen sc1\n\ts_waitcnt vmcnt(0)\n\t"
               "v_cmp_eq_u32 vcc, %0, %4\n\ts_nop 1\n\t"
               "s_cmp_eq_u64 vcc, 0\n\ts_cbranch_scc1 .Lp%=b\n\t"
               "s_sub_u32 %1, %1, 1\n\ts_cbranch_scc1 .Lp%=b\n\t"
               "s_sleep %6\n\ts_branch .Lp%=a\n"
               ".Lp%=b:"
               : "=&v"(v), "=&s"(cnt)
               : "v"(voff), "s"(rsrc), "v"(bad), "i"(SPIN_CAP), "i"(SLEEP)
               : "memory", "scc", "vcc");
}
DEVI void split3(float v, unsigned& h, unsigned& m, unsigned& l) {
  h = pack2(v, 0.f) & 0xffffu;
  const float r1 = v - bflo(h);
  m = pack2(r1, 0.f) & 0xffffu;
  l = pack2(r1 - bflo(m), 0.f) & 0xffffu;
}

DEVI void spin_go(v4i rws) { wait_ne<8>(rws, WS_GO + (blockIdx.x & 7) * 128, 0u); }
DEVI void spin_hb(v4i rws, unsigned hb0) { wait_ne<4>(rws, WS_HB, hb0); }
DEVI void set_flag(v4i rws, unsigned off, int lane) { ast16(v4u{1u, 1u, 1u, 1u}, rws, lane < 4 ? off + lane * 16 : 0x7fffffffu, 0); }

DEVI void pin(float& x) { asm volatile("" : "+v"(x)); }
DEVI void pin(unsigned& x) { asm volatile("" : "+v"(x)); }
DEVI void poison_store(v4i rws, int lane, int wid) {
  const unsigned n = blockIdx.x + 256u * wid;
  ast16(v4u{POISON, POISON, POISON, POISON}, rws, n < (WS_PQ - WS_KVP) / 1024 ? lane * 16 : 0x7fffffffu, WS_KVP + n * 1024);
}

DEVI unsigned wd_voff(int ct2, int lane) { return (unsigned)((lane >> 3) * (D_ * 2) + ct2 * 128 + (((lane & 7) ^ ((lane >> 3) & 6)) * 16)); }
DEVI void wd_coop(v4i rwd, unsigned lds0, unsigned voff, int wid, int k) {
  const int w = wid & 3, n = (wid >> 2) * 6 + k % 6, q = k / 6;
  dma16(rwd, voff, (unsigned)(((w * 12 + q * 3) * 32 + n * 8) * (D_ * 2)), lds0 + slot_grp(q) + w * 3 * SLOT + n * 1024);
}

template <int MODE>
DEVI void phase1(const Params& p, LAS char* shm, const int rt, const int ct, const int ct2, const int mh) {
  constexpr int BM = MODE == 0 ? 160 : 256, BN = MODE == 0 ? 256 : 160;
  constexpr int MT = MODE == 0 ? 5 : 4, NT = MODE == 0 ? 4 : 5;
  constexpr int A_BYTES = BM * 128;
  const int tid = threadIdx.x, lane = tid & 63;
  const int wid = __builtin_amdgcn_readfirstlane(tid >> 6);
  const int wm = MODE == 0 ? (wid >> 2) : (wid >> 1);
  const int wn = MODE == 0 ? (wid & 3) : (wid & 1);
  const int r = lane & 15, g = lane >> 4;
  const int row0 = rt * BM;
  const unsigned lds0 = (unsigned)(uintptr_t)shm;
  const v4i rws = make_rsrc(p.ws, WS_END);

  const int xg = blockIdx.x & 7, xhalf = xg >> 2, xcg = xg & 3;
  const int li = MODE == 0 ? (rt == 6 ? 0 : rt - 7 * xhalf) : (rt & 3);
  const int cls = (xcg + 2 * xhalf + (li & 1)) & 3, kstart = cls == 3 ? 8 : 3 * cls;
  int rmask = 0, cmask, duty;
  if constexpr (MODE == 0) {
    if (rt == 6) { rmask = xhalf ? 0 : (ct % 3 == 0 ? 3 : 4); cmask = 0; }
    else rmask = cmask = 1 << ((ct % 3 + (li >> 1)) % 3);
    duty = ((wn >> 1) == wm) ? 1 + (wn & 1) : 3;
  } else {
    cmask = li < 2 ? 3 : 4;
    duty = wm == 0 ? 3 : 0;
  }
  const int smask = duty == 3 ? cmask : (duty == 0 ? 0 : rmask);
  const int cbase = MODE == 0 ? ct * 256 : (MODE == 1 ? 3072 + ct * 160 : 3712 + ct * 160);

  const v4i rx = make_rsrc(p.x, S_ * D_ * 2);
  const v4i rb0 = MODE == 0 ? make_rsrc(p.wg, D_ * DF_ * 2) : make_rsrc(p.wqkv, D_ * 1920 * 2);
  const v4i rb1 = MODE == 0 ? make_rsrc(p.wu, D_ * DF_ * 2) : rb0;
  const v4i rwd = make_rsrc(p.wd, DF_ * D_ * 2);
  constexpr int BPITCH = MODE == 0 ? DF_ * 2 : 1920 * 2;
  const unsigned va0 = (unsigned)((row0 + 8 * wid + (lane >> 3)) * (D_ * 2) + (((lane & 7) ^ ((4 * wid + (lane >> 4)) & 7)) * 16));
  unsigned vb[3];
  if constexpr (MODE == 0) {
    int k = 8 * wid + (lane >> 3);
    int sig = (k & 2) | ((k >> 1) & 4);
    vb[0] = (unsigned)(k * BPITCH + ct * 256 + ((lane & 7) ^ sig) * 16);
    vb[1] = va0 + 2 * 64 * D_ * 2;
    vb[2] = 0;
  } else {
#pragma unroll
    for (int i = 0; i < 3; i++) {
      int n = (wid + 8 * i) * 64 + lane;
      int k = n / 20, slot = n % 20;
      int c = slot ^ (((k >> 3) & 1) << 1);
      int col;
      if constexpr (MODE == 1) col = 160 * ct + 8 * c;
      else {
        int w2 = c >= 10 ? 1 : 0, cc = c - 10 * w2;
        col = 640 + 80 * ct + 40 * w2 + (8 * cc + 592 * (int)(cc >= 5) + 40 * (int)(cc == 5));
      }
      vb[i] = (unsigned)(k * BPITCH + col * 2);
    }
  }
  const int lomask = (wid >> 2) - 1;
  constexpr unsigned AROWS8 = 64 * D_ * 2;
  auto piece = [&](int s, int i) __attribute__((always_inline)) {
    const unsigned lb = lds0 + (s % 3) * STAGE + wid * 1024;
    const int ks = s + kstart - (s + kstart >= 10 ? 10 : 0);
    const unsigned sa = ks * 128, sb = ks * 64 * BPITCH;
    if constexpr (MODE == 0) {
      if (i == 0) dma16(rx, va0, sa, lb);
      if (i == 1) dma16(rb0, vb[0], sb, lb + A_BYTES);
      if (i == 2) dma16(rb1, vb[0], sb, lb + A_BYTES + 16384);
      if (i == 3) dma16(rx, va0, sa + AROWS8, lb + 8192);
      if (i == 4) dma16(rb0, vb[0], sb + 128, lb + A_BYTES + 8192);
      if (i == 5) dma16(rb1, vb[0], sb + 128, lb + A_BYTES + 24576);
      if (i == 6) dma16m(rx, vb[1], sa, lb + 16384, lomask);
    } else {
      if (i == 0) dma16(rx, va0, sa, lb);
      if (i == 1) dma16(rb0, vb[0], sb, lb + A_BYTES);
      if (i == 2) dma16(rx, va0, sa + AROWS8, lb + 8192);
      if (i == 3) dma16(rx, va0, sa + 2 * AROWS8, lb + 16384);
      if (i == 4) dma16(rb0, vb[1], sb, lb + A_BYTES + 8192);
      if (i == 5) dma16(rx, va0, sa + 3 * AROWS8, lb + 24576);
      if (i == 6) dma16m(rb0, vb[2], sb, lb + A_BYTES + 16384, lomask);
    }
  };
  unsigned hb0 = 0, hb1 = 0, pollv = 0;
  v4u sp[5];
  const int w7mask = negmask(6 - wid);
  const bool stact = wid < 6 && (BM == 256 || wid != 3);
  const int stmask = negmask((int)((BM == 256 ? 0x3fu : 0x37u) << (31 - wid)));
  const unsigned svo = sel((tid & 255) < (wid < 4 ? BM : BN / 2), (unsigned)((tid & 255) * 16), 0x7fffffffu);
  const unsigned sso = wid < 4 ? WS_RP + (unsigned)(row0 * 16) : WS_CP + (unsigned)(cbase * 8), sst = wid < 4 ? 2080 * 16 : 5120 * 8;
  ald4m(hb0, rws, WS_HB, 0, w7mask);
#pragma unroll
  for (int i = 0; i < 7; i++) piece(0, i);
#pragma unroll
  for (int i = 0; i < 7; i++) piece(1, i);
  sched0();

  const int q4 = (lane & 15) >> 2, p4 = lane & 3;
  const int aoff0 = (wm * MT * 16 + r) * 128 + ((g ^ ((r >> 1) & 7)) * 16);
  const int aoff1 = aoff0 ^ 64;
  int boffA, boffB;
  if constexpr (MODE == 0) {
    int sig = (q4 & 2) | ((g & 1) << 2);
    int base = A_BYTES + (wn >> 1) * 8192 + (8 * g + q4) * 128 + (p4 >> 1) * 16 + (p4 & 1) * 8;
    boffA = base + (((4 * (wn & 1)) ^ sig) * 16);
    boffB = base + (((4 * (wn & 1) + 2) ^ sig) * 16);
  } else {
    int base = A_BYTES + (8 * g + q4) * 320 + 160 * wn + (p4 >> 1) * 16 + (p4 & 1) * 8;
    int d = (g & 1) * 32;
    boffA = base + ((wn & 1) ? -d : d);
    boffB = base + ((wn & 1) ? d : -d);
  }
  unsigned lb4[3][4];
#pragma unroll
  for (int q = 0; q < 3; q++) {
    lb4[q][0] = lds0 + q * STAGE + aoff0; lb4[q][1] = lds0 + q * STAGE + aoff1;
    lb4[q][2] = lds0 + q * STAGE + boffA; lb4[q][3] = lds0 + q * STAGE + boffB;
#pragma unroll
    for (int i = 0; i < 4; i++) tie(lb4[q][i]);
  }
  f32x4 acc[MT][NT];
#pragma unroll
  for (int m = 0; m < MT; m++)
#pragma unroll
    for (int n = 0; n < NT; n++) acc[m][n] = f32x4{0.f, 0.f, 0.f, 0.f};
  bf16x8 fa0[MT], fa1[MT];
  bf16x4 fb0[NT][2], fb1[NT][2];
  constexpr int NRD = MT + 2 * NT;
  v4u sl[5], re[MT];
  float rs[MT];

  unsigned obase;
  if constexpr (MODE == 0) obase = WS_H + (unsigned)((((row0 + wm * 80) >> 4) * 48 + ct * 4 + wn) * 1024);
  else if constexpr (MODE == 1) obase = WS_PQ + (unsigned)((((row0 + wm * 64) >> 5) * 40 + ct * 10 + wn * 5) * 1024);
  else obase = 0;
  const unsigned l16 = lane * 16;
  const unsigned wdv = wd_voff(ct2, lane);
  constexpr int TB_L = 0, TB_S = 3456, WO_L = 54272;
  auto rd = [&](bf16x8(&fa)[MT], bf16x4(&fb)[NT][2], int s, int ks, int i) __attribute__((always_inline)) {
    if (i == 0 || i > 2 * NT) {
      const int m = i == 0 ? 0 : i - 2 * NT;
      fa[m] = *(const LAS bf16x8*)((const LAS char*)(uintptr_t)lb4[s % 3][ks] + m * 2048);
    } else {
      const int n = (i - 1) >> 1, h = (i - 1) & 1;
      const LAS char* pb = (const LAS char*)(uintptr_t)lb4[s % 3][2 + (n & 1)];
      if constexpr (MODE == 0) pb += (n >> 1) * 16384 + ks * 4096 + h * 512;
      else pb += n * 32 + ks * 10240 + h * 1280;
      fb[n][h] = __builtin_amdgcn_ds_read_tr16_b64_v4bf16((LAS bf16x4*)pb);
    }
  };
  auto stats = [&](int s) __attribute__((always_inline)) {
    const int ks = s + kstart - (s + kstart >= 10 ? 10 : 0);
    const f32x4 z4 = {0.f, 0.f, 0.f, 0.f};
    if (duty == 3) {
      f32x4 d = z4;
#pragma unroll
      for (int n = 0; n < NT; n++) {
        const unsigned o = r == (n < 4 ? 4 * n : 1) ? 0x3f803f80u : 0u;
        const bf16x8 oh = __builtin_bit_cast(bf16x8, v4u{o, o, o, o});
        d = mfma16(oh, __builtin_shufflevector(fb0[n][0], fb0[n][1], 0, 1, 2, 3, 4, 5, 6, 7), d);
        d = mfma16(oh, __builtin_shufflevector(fb1[n][0], fb1[n][1], 0, 1, 2, 3, 4, 5, 6, 7), d);
      }
      const unsigned so = WS_CP + (unsigned)(((ks >> 1) * 5120 + cbase + wn * NT * 16) * 8 + (ks & 1) * 8);
      const unsigned vo = (unsigned)((lane >> 1) * 16 + (lane & 1) * 4);
      ast4(__float_as_uint(d[0]), rws, vo, so);
      if constexpr (NT == 5) ast4(__float_as_uint(d[1]), rws, lane < 16 ? 512 + vo : 0x7fffffffu, so);
    } else if constexpr (MODE == 0) {
      const bf16x8 ones = __builtin_bit_cast(bf16x8, v4u{0x3f803f80u, 0x3f803f80u, 0x3f803f80u, 0x3f803f80u});
      const unsigned vo = (r >> 2) == g ? r * 16 : 0x7fffffffu;
      auto row = [&](int m) __attribute__((always_inline)) {
        const f32x4 sm = mfma16(ones, fa1[m], mfma16(ones, fa0[m], z4));
        const f32x4 q = mfma16(fa1[m], fa1[m], mfma16(fa0[m], fa0[m], z4));
        float v0 = (r & 1) ? q[1] : q[0], v1 = (r & 1) ? q[3] : q[2];
        pin(v0); pin(v1);
        ast8(v2u{__float_as_uint(sm[0]), __float_as_uint((r & 2) ? v1 : v0)}, rws, vo, WS_RP + (unsigned)(((ks >> 1) * 2080 + row0 + wm * 80 + 16 * m) * 16 + (ks & 1) * 8));
      };
      if (duty == 1) { row(0); row(1); row(2); }
      else { row(3); row(4); }
    }
  };
  auto hook = [&](bf16x8(&fa)[MT], bf16x4(&fb)[NT][2], int s, int h, int idx) __attribute__((always_inline)) {
    if (h == 0 && s == 0) { if (idx % 3 == 1) { piece(2, idx / 3); sched0(); } }
    else if (h == 0) { if (s + 2 <= 9 && idx % 6 == 1 && idx / 6 < 3) { piece(s + 2, 4 + idx / 6); sched0(); } }
    else { if (s + 3 <= 9 && idx % 5 == 1) { piece(s + 3, idx / 5); sched0(); } }
    if (s == 0 && h == 1 && idx == 3) { poison_store(rws, lane, wid); sched0(); }
    if (s < 3 && h == 0 && idx == 19) {
      if (__builtin_expect((smask >> s) & 1, 0)) stats(s);
      sched0();
    }
    if (s == 2 && h == 0 && idx == 18) { ald4m(hb1, rws, WS_HB, 0, w7mask); sched0(); }
    if (s == 3 && h == 1 && idx == 0) {
      tie(hb0);
      tie(hb1);
      if (__builtin_expect(wid == 7 && __builtin_amdgcn_readfirstlane(hb0) == __builtin_amdgcn_readfirstlane(hb1), 0)) spin_hb(rws, hb0);
      ast16m(v4u{1u, 1u, 1u, 1u}, rws, lane < 4 ? lane * 16 : 0x7fffffffu, WS_SIDE + blockIdx.x * 64, w7mask);
      sched0();
    }
    if (s == 6 && h == 1 && idx == 0) {
      ald4m(pollv, rws, 0u, WS_GO + (blockIdx.x & 7) * 128, negmask(wid - 1));
      sched0();
    }
    if (s == 7 && h == 1 && idx % 4 == 2) {
      sp[idx / 4] = v4u{0u, 0u, 0u, 0u};
      ald16m(sp[idx / 4], rws, svo, sso + (idx / 4) * sst, stmask);
      sched0();
    }
    if (s == 9 && h == 0 && idx == 2) {
      wait_vm<3>();
      f32x4 a4 = {0.f, 0.f, 0.f, 0.f};
#pragma unroll
      for (int i = 0; i < 5; i++) { tie(sp[i]); a4 += __builtin_bit_cast(f32x4, sp[i]); }
      const f32x2 a2 = {a4[0] + a4[2], a4[1] + a4[3]};
      unsigned h3, m3, l3;
      if (wid < 4) {
        const float mu = a2[0] * (1.f / D_);
        const float rstd = __builtin_amdgcn_rsqf(fmaxf(a2[1] * (1.f / D_) - mu * mu, 0.f) + 1e-5f);
        split3(-mu, h3, m3, l3);
        *(LAS v4u*)(shm + T_ROW + tid * 16) = v4u{h3 | (h3 << 16), h3 | (m3 << 16), m3 | (l3 << 16), __float_as_uint(rstd)};
      } else {
        LAS char* dst = shm + T_COL + ((tid - 256) & 127) * 32;
        if (wid < 6) {
#pragma unroll
          for (int q = 0; q < 2; q++) {
            split3(a2[q], h3, m3, l3);
            *(LAS v4u*)(dst + q * 16) = v4u{h3 | (m3 << 16), l3 | (h3 << 16), m3 | (h3 << 16), 0u};
          }
        }
      }
      *(LAS unsigned*)(shm + T_CNT + (tid & 15) * 4) = 0u;
      sched0();
    }
    if (s == 9 && h == 1 && idx == 0) {
#pragma unroll
      for (int n = 0; n < NT; n++) sl[n] = *(const LAS v4u*)(shm + T_COL + (wn * NT * 16 + 16 * n + r) * 16);
#pragma unroll
      for (int m = 0; m < MT; m++) re[m] = *(const LAS v4u*)(shm + T_ROW + (wm * MT * 16 + 16 * m + r) * 16);
      sched0();
    }
    if (s == 9 && h == 1 && idx % NT == NT - 1) {
      const int m = idx / NT;
      const unsigned gm = g == 0 ? 0xffffffffu : 0u;
      rs[m] = __uint_as_float(re[m][3]);
      const v4u rf = {re[m][0] & gm, re[m][1] & gm, re[m][2] & gm, 0u};
#pragma unroll
      for (int n = 0; n < NT; n++) acc[m][n] = MODE == 2 ? mfma16(rf, sl[n], acc[m][n]) : mfma16(sl[n], rf, acc[m][n]);
      sched0();
    }
    {
      int k = -1;
      const int q = s * 2 + h - 15;
      if (q >= 0 && q < 4 && idx >= 8 && idx % 4 == 0) k = q * 3 + (idx - 8) / 4;
      if constexpr (MODE == 1) {
        if (k >= 0) {
          const int pc = min((k & 3) * 8 + wid, 24), n = pc * 64 + lane, row = n / 80, c = n % 80;
          dma16(make_rsrc(p.wo, D_ * D_ * 2), (unsigned)(row * 1280 + (c ^ ((row & 7) << 1)) * 16), (unsigned)(mh * 25600), lds0 + WO_L + pc * 1024);
          sched0();
        }
      } else if (k >= 0) { wd_coop(rwd, lds0, wdv, wid, k); sched0(); }
    }
  };
  auto mma = [&](bf16x8(&fa)[MT], bf16x4(&fb)[NT][2], bf16x8(&ga)[MT], bf16x4(&gb)[NT][2], int s, int h) __attribute__((always_inline)) {
    static_for<0, 20>([&](auto I) __attribute__((always_inline)) {
      constexpr int idx = decltype(I)::value, m = idx / NT, n = idx % NT;
      if (s + h <= 9 && idx < NRD) rd(ga, gb, s + h, 1 - h, idx);
      bf16x8 b = __builtin_shufflevector(fb[n][0], fb[n][1], 0, 1, 2, 3, 4, 5, 6, 7);
      if constexpr (MODE == 2) acc[m][n] = mfma16(fa[m], b, acc[m][n]);
      else acc[m][n] = mfma16(b, fa[m], acc[m][n]);
      sched0();
      hook(fa, fb, s, h, idx);
    });
  };

  wait_vm<6>();
  bar();
#pragma unroll
  for (int i = 0; i < NRD; i++) rd(fa0, fb0, 0, 0, i);
  sched0();
  static_for<0, 10>([&](auto SI) __attribute__((always_inline)) {
    constexpr int s = decltype(SI)::value;
    mma(fa0, fb0, fa1, fb1, s, 0);
    if (s == 1) wait_vm<7>();
    else if (s == 8) { if (stact) wait_vm<11>(); else wait_vm<6>(); }
    else if (s < 9) wait_vm<6>();
    if (s == 7) {
      tie(pollv);
      if (__builtin_expect(wid == 0 && __builtin_amdgcn_readfirstlane(pollv) == 0, 0)) spin_go(rws);
    }
    wait_lgkm0();
    bar();
    sched0();
    mma(fa1, fb1, fa0, fb0, s, 1);
  });
  constexpr float LOG2E = 1.4426950408889634f;
  if constexpr (MODE == 0) {
    constexpr float C0 = -2.f * 0.7978845608028654f * LOG2E, C1 = C0 * 0.044715f;
#pragma unroll
    for (int m = 0; m < MT; m++) {
      const float a = rs[m], a2 = a * a, k0 = C0 * a, k1 = C1 * a * a2, iv = __builtin_amdgcn_rcpf(a2);
      const f32x4 g0 = acc[m][0], g1 = acc[m][1];
      const f32x4 e0 = exp2v(g0 * (g0 * g0 * k1 + k0)), e1 = exp2v(g1 * (g1 * g1 * k1 + k0));
      const f32x4 h0 = (g0 * acc[m][2]) * rcpv(e0 * iv + iv), h1 = (g1 * acc[m][3]) * rcpv(e1 * iv + iv);
      const v2u p0 = pack4(h0), p1 = pack4(h1);
      ast16(v4u{p0.x, p0.y, p1.x, p1.y}, rws, row0 + wm * 80 + 16 * m < S_ ? l16 : 0x7fffffffu, obase + m * 48 * 1024);
      sched0();
    }
  } else if constexpr (MODE == 1) {
    v2u pb[MT][5];
#pragma unroll
    for (int m = 0; m < MT; m++)
#pragma unroll
      for (int t = 0; t < 5; t++) {
        acc[m][t] = phis(acc[m][t], f32x4{rs[m], rs[m], rs[m], rs[m]}, f32x4{rs[m], rs[m], rs[m], rs[m]} * LOG2E);
        pb[m][t] = pack4(acc[m][t]);
        asm volatile("" : "+v"(acc[m][t]), "+v"(pb[m][t]));
      }
    v4u ga[2], gb[2];
    const unsigned gvo0 = WS_KVP + (unsigned)((mh * 8 + (wid & 3) * 2) * 1664 + lane * 16);
    const unsigned gvo1 = WS_KVP + (unsigned)((mh * 8 + (wid & 3) * 2) * 1664 + min(lane + 64, 99) * 16);
    {
      const unsigned vo = (unsigned)(g * 4096 + r * 16), so = WS_ZP + (unsigned)((wid >> 2) * 16 * 4096 + (ct * 4 + (wid & 3)) * 256);
      v4u a[4];
      auto ldz = [&]() __attribute__((always_inline)) {
#pragma unroll
        for (int k = 0; k < 4; k++) ald16(a[k], rws, vo, so + k * 4 * 4096);
        wait_vm<0>();
        unsigned mx = 0;
#pragma unroll
        for (int k = 0; k < 4; k++) { tie(a[k]); mx = max(mx, umax4(a[k])); }
        return __all(mx != POISON);
      };
      int left = SPIN_CAP;
#pragma nounroll
      do left = ldz() ? 0 : left - 1; while (left > 0);
#pragma unroll
    for (int k = 0; k < 2; k++) {
      ga[k] = gb[k] = v4u{0u, 0u, 0u, 0u};
      ald16m(ga[k], rws, gvo0, k * 1664, lomask);
      ald16m(gb[k], rws, gvo1, k * 1664, lomask);
    }
      f32x4 zs = (__builtin_bit_cast(f32x4, a[0]) + __builtin_bit_cast(f32x4, a[1])) + (__builtin_bit_cast(f32x4, a[2]) + __builtin_bit_cast(f32x4, a[3]));
#pragma unroll
      for (int j = 0; j < 4; j++) zs[j] = sum32(sum16(zs[j]));
      *(LAS f32x4*)(shm + T_Z + wid * 256 + r * 16) = zs;
      wait_lgkm0();
      lds_inc(shm, 0);
      lds_wait(shm, 0, 8u);
    }
    if (wid < 4) {
      LAS float* kvs = (LAS float*)(shm + TB_L);
      {
        wait_vm<0>();
        auto okP = [&]() __attribute__((always_inline)) {
          tie(ga[0]); tie(ga[1]); tie(gb[0]); tie(gb[1]);
          return __all(max(max(umax4(ga[0]), umax4(ga[1])), max(umax4(gb[0]), umax4(gb[1]))) != POISON);
        };
        if (__builtin_expect(!okP(), 0)) {
#pragma nounroll
          for (unsigned it = 0; it < SPIN_CAP; it++) {
#pragma unroll
            for (int k = 0; k < 2; k++) { ald16(ga[k], rws, gvo0, k * 1664); ald16(gb[k], rws, gvo1, k * 1664); }
            wait_vm<0>();
            if (okP()) break;
            __builtin_amdgcn_s_sleep(2);
          }
        }
        const f32x4 s0 = __builtin_bit_cast(f32x4, ga[0]) + __builtin_bit_cast(f32x4, ga[1]);
        const f32x4 s1 = __builtin_bit_cast(f32x4, gb[0]) + __builtin_bit_cast(f32x4, gb[1]);
        *(LAS f32x4*)(kvs + wid * 512 + lane * 4) = s0;
        *(LAS f32x4*)(kvs + wid * 512 + 256 + lane * 4) = s1;
        wait_lgkm0();
        lds_inc(shm, 1);
      }
      bf16x8 wb[5][2];
      {
        const int r0 = min(8 * g, 16) + q4, r1 = min(8 * g + 4, 16) + q4;
#pragma unroll
        for (int k = 0; k < 10; k++) {
          const int c = wid * 20 + 2 * k + (p4 >> 1);
          wb[k >> 1][k & 1] = tr_frag(shm + WO_L + r0 * 1280 + (c ^ ((r0 & 7) << 1)) * 16 + (p4 & 1) * 8,
                                      shm + WO_L + r1 * 1280 + (c ^ ((r1 & 7) << 1)) * 16 + (p4 & 1) * 8);
        }
      }
      lds_wait(shm, 1, 4u);
      v4u ah[2], al[2];
#pragma unroll
      for (int dt = 0; dt < 2; dt++) {
        const int d = 16 * dt + r;
        const LAS float* src = kvs + min(d, 19) * 20 + min(8 * g, 16);
        f32x4 f0 = (*(const LAS f32x4*)src + *(const LAS f32x4*)(src + 512)) + (*(const LAS f32x4*)(src + 1024) + *(const LAS f32x4*)(src + 1536));
        f32x4 f1 = (*(const LAS f32x4*)(src + 4) + *(const LAS f32x4*)(src + 516)) + (*(const LAS f32x4*)(src + 1028) + *(const LAS f32x4*)(src + 1540));
        f0 = __builtin_bit_cast(f32x4, __builtin_bit_cast(v4u, f0) & ((d < 20 && g < 3) ? 0xffffffffu : 0u));
        f1 = __builtin_bit_cast(f32x4, __builtin_bit_cast(v4u, f1) & ((d < 20 && g < 2) ? 0xffffffffu : 0u));
        const v2u h0 = pack4(f0), h1 = pack4(f1);
        const v2u l0 = pack4(f0 - f32x4{bflo(h0.x), bfhi(h0.x), bflo(h0.y), bfhi(h0.y)});
        const v2u l1 = pack4(f1 - f32x4{bflo(h1.x), bfhi(h1.x), bflo(h1.y), bfhi(h1.y)});
        ah[dt] = v4u{h0.x, h0.y, h1.x, h1.y};
        al[dt] = v4u{l0.x, l0.y, l1.x, l1.y};
      }
      const unsigned mv0 = (unsigned)(g * 5120 + r * 16), mv1 = g == 0 ? (unsigned)(4 * 5120 + r * 16) : 0x7fffffffu;
#pragma unroll
      for (int k = 0; k < 5; k++) {
        const unsigned so = WS_MF + (unsigned)((mh * 100 + wid * 5 + k) * 256);
#pragma unroll
        for (int dt = 0; dt < 2; dt++) {
          f32x4 c0 = {0.f, 0.f, 0.f, 0.f}, c1 = c0;
          c0 = mfma16(__builtin_bit_cast(bf16x8, al[dt]), wb[k][0], c0);
          c1 = mfma16(__builtin_bit_cast(bf16x8, al[dt]), wb[k][1], c1);
          c0 = mfma16(__builtin_bit_cast(bf16x8, ah[dt]), wb[k][0], c0);
          c1 = mfma16(__builtin_bit_cast(bf16x8, ah[dt]), wb[k][1], c1);
          const v4u o = {pack2s(c0[0], c0[1]), pack2s(c0[2], c0[3]), pack2s(c1[0], c1[1]), pack2s(c1[2], c1[3])};
          ast16(o, rws, dt == 0 ? mv0 : mv1, so);
        }
      }
    }
    v2u zh[6], zl[6];
#pragma unroll
    for (int t = 0; t < 5; t++) {
      const int k80 = 16 * t + 4 * g, hp = k80 >= 40 ? 1 : 0;
      const LAS char* src = shm + T_Z + (2 * wn + hp) * 256 + (k80 - 40 * hp) * 4;
      f32x4 z = *(const LAS f32x4*)src + *(const LAS f32x4*)(src + 1024);
      z = __builtin_bit_cast(f32x4, __builtin_bit_cast(v4u, z) & ((4 * t + g) / 5 == (r & 3) ? 0xffffffffu : 0u));
      zh[t] = pack4(z);
      zl[t] = pack4(z - f32x4{bflo(zh[t].x), bfhi(zh[t].x), bflo(zh[t].y), bfhi(zh[t].y)});
    }
    zh[5] = zl[5] = v2u{0u, 0u};
    f32x4 dn[MT];
#pragma unroll
    for (int m = 0; m < MT; m++) {
      f32x4 d = {1e-6f, 1e-6f, 1e-6f, 1e-6f};
#pragma unroll
      for (int k = 0; k < 3; k++) {
        const v2u b1 = pb[m][k < 2 ? 2 * k + 1 : 4];
        const v4u b = {pb[m][2 * k].x, pb[m][2 * k].y, b1.x, b1.y};
        d = mfma16(v4u{zl[2 * k].x, zl[2 * k].y, zl[2 * k + 1].x, zl[2 * k + 1].y}, b, d);
        d = mfma16(v4u{zh[2 * k].x, zh[2 * k].y, zh[2 * k + 1].x, zh[2 * k + 1].y}, b, d);
      }
      dn[m] = d;
    }
    v2u o[5];
#pragma unroll
    for (int m = 0; m < MT; m++) {
      const f32x4 rc = rcpv(dn[m]);
      const float f[5] = {rc[0], g == 0 ? rc[0] : rc[1], g <= 1 ? rc[1] : rc[2], g <= 2 ? rc[2] : rc[3], rc[3]};
#pragma unroll
      for (int t = 0; t < 5; t++) {
        const v2u pk = pack4(acc[m][t] * f[t]);
        if (m & 1) ast16(v4u{o[t].x, o[t].y, pk.x, pk.y}, rws, l16, obase + ((m >> 1) * 40 + t) * 1024);
        else o[t] = pk;
      }
      sched0();
    }
    wait_vm<0>();
    lds_inc(shm, 5);
    if (wid == 3) {
      lds_wait(shm, 5, 8u);
      set_flag(rws, WS_QF + (unsigned)((rt * 4 + ct) * 64), lane);
    }
  } else {
    v4u A0[2], A1[2], A2[2], B2[2], B3[2], B4[2];
    f32x4 rs4[4];
#pragma unroll
    for (int m = 0; m < 4; m++)
#pragma unroll
      for (int j = 0; j < 4; j++) rs4[m][j] = *(const LAS float*)(shm + T_ROW + (wm * 64 + 16 * m + 4 * g + j) * 16 + 12);
    auto prod = [&](v4u(&a)[2], v4u(&b)[2]) __attribute__((always_inline)) -> f32x4 {
      f32x4 c = {0.f, 0.f, 0.f, 0.f};
      c = mfma16(a[0], b[0], c);
      c = mfma16(a[1], b[1], c);
      return c;
    };
#pragma unroll
    for (int m = 0; m < 4; m++) {
      const int u = m >> 1, o = (m & 1) * 2;
      const f32x4 rl = rs4[m] * LOG2E;
      const v2u a0 = pack4(phis(acc[m][0], rs4[m], rl)), a1 = pack4(phis(acc[m][1], rs4[m], rl)), a2 = pack4(phis(acc[m][2], rs4[m], rl));
      A0[u][o] = a0.x; A0[u][o + 1] = a0.y;
      A1[u][o] = a1.x; A1[u][o + 1] = a1.y;
      A2[u][o] = a2.x; A2[u][o + 1] = a2.y;
    }
    {
      v4u ones[2] = {{0x3f803f80u, 0x3f803f80u, 0x3f803f80u, 0x3f803f80u}, {0x3f803f80u, 0x3f803f80u, 0x3f803f80u, 0x3f803f80u}};
      const float z0 = prod(ones, A0)[0], z1 = prod(ones, A1)[0], z2 = prod(ones, A2)[0];
      const float zz = g == 0 ? z0 : (g == 1 ? z1 : z2);
      ast4(nopoison(__float_as_uint(zz)), rws, lane * 4, WS_ZP + (unsigned)(((rt * 4 + wm) * 16 + ct * 2 + wn) * 256));
      sched0();
    }
#pragma unroll
    for (int m = 0; m < 4; m++) {
      const int u = m >> 1, o = (m & 1) * 2;
      const v2u b2 = pack4(acc[m][2] * rs4[m]), b3 = pack4(acc[m][3] * rs4[m]), b4 = pack4(acc[m][4] * rs4[m]);
      B2[u][o] = b2.x; B2[u][o + 1] = b2.y;
      B3[u][o] = b3.x; B3[u][o + 1] = b3.y;
      B4[u][o] = b4.x; B4[u][o + 1] = b4.y;
    }
    LAS float* tb = (LAS float*)(shm + TB_L + wid * TB_S);
    auto put = [&](f32x4 d, int kt, int vt) __attribute__((always_inline)) {
      const int kc = 16 * kt + r, hh = kc >= 20 ? 1 : 0, dd = kc - 20 * hh;
      const int vq = vt == 3 ? 4 * g : (vt == 4 ? 16 + 4 * g : 24 + 4 * g);
      const int vh = vq >= 20 ? 1 : 0;
      const bool kok = kc < 40;
      const bool ok = kok & (vh == hh) & (vt != 2 || g >= 2);
      *(LAS f32x4*)(tb + sel(ok, hh * 420 + dd * 20 + vq - 20 * vh, 840)) = d;
    };
    put(prod(B3, A0), 0, 3);
    put(prod(B4, A0), 0, 4);
    put(prod(B3, A1), 1, 3);
    put(prod(B4, A1), 1, 4);
    put(prod(B2, A1), 1, 2);
    put(prod(B4, A2), 2, 4);
    put(prod(B2, A2), 2, 2);
    wait_lgkm0();
    bar();
    {
      const int t4 = min(tid, 415), hl = t4 / 104, e = t4 % 104;
      f32x4 s = {0.f, 0.f, 0.f, 0.f};
#pragma unroll
      for (int w = 0; w < 4; w++) s += *(const LAS f32x4*)(shm + TB_L + (w * 2 + (hl >> 1)) * TB_S + (hl & 1) * 1680 + e * 16);
      v4u o;
#pragma unroll
      for (int j = 0; j < 4; j++) o[j] = nopoison(__float_as_uint(s[j]));
      ast16(o, rws, sel(tid < 416, WS_KVP + (unsigned)(((4 * ct + hl) * 8 + rt) * 1664 + e * 16), 0x7fffffffu), 0);
    }
    wait_lgkm0();
    lds_inc(shm, 6);
  }
}

#define FINISH do { asm volatile("s_endpgm" ::: "memory"); __builtin_amdgcn_endpgm(); } while (0)
DEVI v4u ldsc(__amdgpu_buffer_rsrc_t r, unsigned voff, unsigned soff) { return __builtin_amdgcn_raw_buffer_load_b128(r, voff, soff, 16); }

template <bool KVB>
DEVI void phase2(const Params& p, LAS char* shm, const int rt, const int ct, const unsigned myflag) {
  const int tid = threadIdx.x, lane = tid & 63;
  const int wid = __builtin_amdgcn_readfirstlane(tid >> 6);
  const int r = lane & 15, g = lane >> 4;
  const unsigned lds0 = (unsigned)(uintptr_t)shm;
  const __amdgpu_buffer_rsrc_t rws = __builtin_amdgcn_make_buffer_rsrc(p.ws, 0, WS_END, 0x00020000);
  const unsigned l16 = lane * 16;
  f32x4 acc[6][4];
  const f32x4 zero4 = {0.f, 0.f, 0.f, 0.f};
  const v4i rwsa = make_rsrc(p.ws, WS_END);
  const int w = wid & 3;
  const int rlast = min(rt * 96 + 95, S_ - 1);
  int rt16[6];
#pragma unroll
  for (int m = 0; m < 6; m++) rt16[m] = min(rt * 6 + m, 127);
  LAS char* xa = shm + slot_addr(w, 0) + l16;
  LAS char* xb = shm + slot_addr(w, 3) + l16;

  if (wid < 4) {
    const v4i rwd = make_rsrc(p.wd, DF_ * D_ * 2);
    const unsigned wdv = wd_voff(ct, lane);
    const int q4 = (lane & 15) >> 2, p4 = lane & 3;
    unsigned bb[4];
#pragma unroll
    for (int n = 0; n < 4; n++) {
      bb[n] = lds0 + w * 3 * SLOT + (4 * g + q4) * 128 + (((2 * n + (p4 >> 1)) ^ ((4 * g + q4) & 6)) * 16) + (p4 & 1) * 8;
      pin(bb[n]);
    }
    auto rdB = [&](bf16x8(&fb)[4], int sj) __attribute__((always_inline)) {
#pragma unroll
      for (int n = 0; n < 4; n++) {
        const LAS char* b = (const LAS char*)(uintptr_t)bb[n] + slot_grp(sj / 3) + (sj % 3) * SLOT;
        fb[n] = tr_frag(b, b + 2048);
      }
    };
    bf16x8 br[3][4];
#pragma unroll
    for (int j = 0; j < 3; j++) rdB(br[j], j);
    wait_vm<0>();
    wait_lgkm0();
    lds_inc(shm, 5);
#pragma unroll
    for (int j = 0; j < 3; j++)
#pragma unroll
      for (int n = 0; n < 4; n++) asm volatile("" : "+v"(br[j][n]));
    sched0();
    if constexpr (KVB) {
      lds_wait(shm, 6, 8u);
    }
    {
      const int mr = (lane & 4) ? rlast / 160 : (rt * 96) / 160;
      wait_ne<4>(rwsa, WS_HF + (unsigned)((mr * 12 + w * 3 + min(lane & 3, 2)) * 64), 0u);
    }
    v4u fa[12][6];
    auto issA = [&](int j, int m) __attribute__((always_inline)) {
      fa[j][m] = ldsc(rws, l16, WS_H + (unsigned)((rt16[m] * 48 + w * 12 + j) * 1024));
    };
#pragma unroll
    for (int m = 0; m < 6; m++)
#pragma unroll
      for (int j = 0; j < 3; j++) issA(j, m);
#pragma unroll
    for (int j = 3; j < 6; j++)
#pragma unroll
      for (int m = 0; m < 6; m++) issA(j, m);
    sched0();
#pragma unroll
    for (int m = 0; m < 6; m++) {
#pragma unroll
      for (int j = 0; j < 3; j++)
#pragma unroll
        for (int n = 0; n < 4; n++) acc[m][n] = mfma16(br[j][n], __builtin_bit_cast(bf16x8, fa[j][m]), j == 0 ? zero4 : acc[m][n]);
      sched0();
    }
    lds_wait(shm, 5, 8u);
    bf16x4 fb[2][4][2];
    auto rdB1 = [&](int j, int i) __attribute__((always_inline)) {
      const int sj = j < 9 ? j : j - 9;
      if (i == 0 && (j == 3 || j == 6)) {
#pragma unroll
        for (int n = 0; n < 4; n++) { bb[n] += j == 3 ? slot_grp(1) : -slot_grp(1); pin(bb[n]); }
      }
      const LAS char* b = (const LAS char*)(uintptr_t)bb[i >> 1] + (sj / 3 == 0 ? slot_grp(0) : 0) + (sj % 3) * SLOT + (i & 1) * 2048;
      fb[j & 1][i >> 1][i & 1] = __builtin_amdgcn_ds_read_tr16_b64_v4bf16((LAS bf16x4*)b);
    };
#pragma unroll
    for (int i = 0; i < 8; i++) rdB1(3, i);
#pragma unroll
    for (int m = 0; m < 6; m++) issA(6, m);
    sched0();
    static_for<3, 12>([&](auto J) __attribute__((always_inline)) {
      constexpr int j = decltype(J)::value;
      if (j == 5) lds_wait(shm, 11, 4u);
      static_for<0, 24>([&](auto I) __attribute__((always_inline)) {
        constexpr int idx = decltype(I)::value, m = idx >> 2, n = idx & 3;
        if (j + 1 < 12 && idx < 8) rdB1(j + 1, idx);
        const bf16x8 b = __builtin_shufflevector(fb[j & 1][n][0], fb[j & 1][n][1], 0, 1, 2, 3, 4, 5, 6, 7);
        acc[m][n] = mfma16(b, __builtin_bit_cast(bf16x8, fa[j][m]), acc[m][n]);
        if (n == 3 && j + 4 < 12) issA(j + 4, m);
        sched0();
      });
    });
    const __amdgpu_buffer_rsrc_t rxi = __builtin_amdgcn_make_buffer_rsrc((void*)p.x, 0, S_ * D_ * 2, 0x00020000);
    const __amdgpu_buffer_rsrc_t ro = __builtin_amdgcn_make_buffer_rsrc(p.out, 0, S_ * D_ * 2, 0x00020000);
    const unsigned ooff = (unsigned)(((rt * 96 + r) * D_ + ct * 64 + w * 16 + 4 * g) * 2);
    v2u xv[6];
#pragma unroll
    for (int m = 0; m < 6; m++) xv[m] = __builtin_amdgcn_raw_buffer_load_b64(rxi, ooff + m * 16 * D_ * 2, 0, 0);
#pragma unroll
    for (int m = 0; m < 6; m++)
#pragma unroll
      for (int n = 0; n < 4; n++) *(LAS f32x4*)((m < 3 ? xa + m * SLOT : xb + (m - 3) * SLOT) + n * 1024) = acc[m][n];
    wait_lgkm0();
    lds_inc(shm, 2);
    lds_wait(shm, 2, 4u);
    auto gather = [&](f32x4(&o)[6]) __attribute__((always_inline)) {
      f32x4 q[4][6];
      const LAS char* src = shm + w * 1024 + l16;
#pragma unroll
      for (int v = 0; v < 4; v++)
#pragma unroll
        for (int m = 0; m < 6; m++) q[v][m] = *(const LAS f32x4*)(src + slot_addr(v, m));
#pragma unroll
      for (int m = 0; m < 6; m++) o[m] = (q[0][m] + q[1][m]) + (q[2][m] + q[3][m]);
    };
    f32x4 gt[6];
    gather(gt);
#pragma unroll
    for (int m = 0; m < 6; m++) asm volatile("" : "+v"(gt[m]));
    wait_lgkm0();
    lds_inc(shm, 3);
#pragma unroll
    for (int m = 0; m < 6; m++) gt[m] = gt[m] * rcpv(exp2v(gt[m] * -1.4426950408889634f) + 1.f);
#pragma unroll
    for (int m = 0; m < 6; m++) asm volatile("" : "+v"(gt[m]));
#pragma unroll
    for (int mp = 0; mp < 3; mp++) {
      lds_wait(shm, 8 + mp, 4u);
      const LAS char* src = shm + w * 1024 + l16;
      f32x4 q[2][4];
#pragma unroll
      for (int k = 0; k < 2; k++)
#pragma unroll
        for (int v = 0; v < 4; v++) q[k][v] = *(const LAS f32x4*)(src + slot_addr(v, 2 * mp + k));
#pragma unroll
      for (int k = 0; k < 2; k++) {
        const int m = 2 * mp + k;
        const f32x4 sv = (q[k][0] + q[k][1]) + (q[k][2] + q[k][3]);
        const f32x4 xx = {bflo(xv[m].x), bfhi(xv[m].x), bflo(xv[m].y), bfhi(xv[m].y)};
        __builtin_amdgcn_raw_buffer_store_b64(pack4(xx + sv * gt[m]), ro, ooff + m * 16 * D_ * 2, 0, 0);
      }
    }
    FINISH;
  } else {
    wait_vm<0>();
    lds_inc(shm, 5);
    if (!KVB && wid == 7 && myflag != 0u) {
      lds_wait(shm, 5, 8u);
      set_flag(rwsa, myflag, lane);
    }
    {
      lds_wait(shm, 5, 8u);
      if constexpr (KVB) lds_wait(shm, 6, 8u);
      const v4i rwd = make_rsrc(p.wd, DF_ * D_ * 2);
      const unsigned wdv = wd_voff(ct, lane);
#pragma unroll
      for (int n = 0; n < 12; n++) dma16(rwd, wdv, (unsigned)(((w * 12 + 6) * 32 + n * 8) * (D_ * 2)), lds0 + slot_addr(w, 6) + n * 1024);
#pragma unroll
      for (int n = 0; n < 12; n++) dma16(rwd, wdv, (unsigned)(((w * 12 + 9) * 32 + n * 8) * (D_ * 2)), lds0 + slot_addr(w, 0) + n * 1024);
      wait_vm<0>();
      lds_inc(shm, 11);
    }
    v4u la[3][10], lm[5][2][2];
    const unsigned mvo = (unsigned)(g * 5120 + r * 16);
    auto issM = [&](int jj) __attribute__((always_inline)) {
#pragma unroll
      for (int hf = 0; hf < 2; hf++)
#pragma unroll
        for (int pr = 0; pr < 2; pr++) lm[jj][hf][pr] = ldsc(rws, mvo, WS_MF + (unsigned)((((w * 5 + jj) * 8 + 4 * hf) * 20 + ct * 2 + pr) * 256));
    };
    wait_ne<4>(rwsa, WS_MF + (unsigned)((((w * 8 + (lane & 7)) * 5 + 4) * 20 + ct * 2 + ((lane >> 3) & 1)) * 256), POISON);
#pragma unroll
    for (int jj = 0; jj < 5; jj++) issM(jj);
    sched0();
    wait_ne<2>(rwsa, WS_QF + (unsigned)(((((lane & 1) ? rlast : rt * 96) >> 8) * 4 + w) * 64), 0u);
#pragma unroll
    for (int mp = 0; mp < 3; mp++)
#pragma unroll
      for (int T = 0; T < 10; T++) la[mp][T] = ldsc(rws, l16, WS_PQ + (unsigned)((min(rt * 3 + mp, 63) * 40 + w * 10 + T) * 1024));
    sched0();
    {
      auto okM = [&]() __attribute__((always_inline)) {
        unsigned mx = 0;
#pragma unroll
        for (int jj = 0; jj < 5; jj++)
          mx = max(mx, max(max(umax4(lm[jj][0][0]), umax4(lm[jj][0][1])), max(umax4(lm[jj][1][0]), umax4(lm[jj][1][1]))));
        return __all(mx != POISON);
      };
      if (__builtin_expect(!okM(), 0)) {
#pragma nounroll
        for (unsigned it = 0; it < SPIN_CAP; it++) {
          __builtin_amdgcn_s_sleep(4);
#pragma unroll
          for (int jj = 0; jj < 5; jj++) issM(jj);
          if (okM()) break;
        }
      }
    }
#pragma unroll
    for (int jj = 0; jj < 5; jj++) { xhalf(lm[jj][0][0], lm[jj][1][0]); xhalf(lm[jj][0][1], lm[jj][1][1]); }
    sched0();
#pragma unroll
    for (int mp = 0; mp < 3; mp++) {
#pragma unroll
      for (int jj = 0; jj < 5; jj++) {
        xhalf(la[mp][2 * jj], la[mp][2 * jj + 1]);
#pragma unroll
        for (int n = 0; n < 4; n++) {
          acc[2 * mp][n] = mfma16(lm[jj][n & 1][n >> 1], la[mp][2 * jj], jj == 0 ? zero4 : acc[2 * mp][n]);
          acc[2 * mp + 1][n] = mfma16(lm[jj][n & 1][n >> 1], la[mp][2 * jj + 1], jj == 0 ? zero4 : acc[2 * mp + 1][n]);
        }
        if (mp > 0 && jj < 4) {
          if (mp == 1 && jj == 0) lds_wait(shm, 3, 4u);
#pragma unroll
          for (int q = 0; q < 2; q++) {
            const int m = 2 * (mp - 1) + q;
            *(LAS f32x4*)((m < 3 ? xa + m * SLOT : xb + (m - 3) * SLOT) + jj * 1024) = acc[m][jj];
          }
        }
        if (mp > 0 && jj == 4) { wait_lgkm0(); lds_inc(shm, 8 + mp - 1); }
        sched0();
      }
    }
#pragma unroll
    for (int m = 4; m < 6; m++)
#pragma unroll
      for (int n = 0; n < 4; n++) *(LAS f32x4*)(xb + (m - 3) * SLOT + n * 1024) = acc[m][n];
    wait_lgkm0();
    lds_inc(shm, 10);
    FINISH;
  }
}

__global__ __launch_bounds__(512, 2) void fused(Params p) {
  extern __shared__ __attribute__((aligned(16))) char shm_[];
  LAS char* shm = (LAS char*)shm_;
  const int b = blockIdx.x;
  const int x = b & 7, j = b >> 3, half = x >> 2, cg = x & 3;
  const int idx = j - 20, ty = j >= 20 ? idx % 3 : -1;
  const int rank = j < 20 ? j : (ty == 0 ? 99 : 16 + 4 * ty + idx / 3);
  const int t2 = ((55 * x) >> 1) + rank;
  const bool has2 = t2 < ((55 * (x + 1)) >> 1);
  const int rt2 = has2 ? t2 / 10 : 0, ct2 = has2 ? t2 % 10 : 0;
  const int lane = threadIdx.x & 63;
  const int wid = __builtin_amdgcn_readfirstlane(threadIdx.x >> 6);
  unsigned pf0;
  {
    uint64_t pc;
    asm volatile("s_getpc_b64 %0" : "=s"(pc));
    const unsigned off = (unsigned)((j * 8 + wid) * 8 + (lane & 7)) * 128u;
    asm volatile("global_load_dword %0, %1, %2" : "=v"(pf0) : "v"(off < CODEPF ? off : 0u), "s"(pc & ~127ull) : "memory");
  }
  if (j < 18 || (j < 20 && (half == 0 || j == 18))) {
    const int rt = j < 18 ? half * 7 + j / 3 : 6;
    const int ct = 3 * cg + (j < 18 ? j % 3 : (half == 0 ? j - 18 : 2));
    phase1<0>(p, shm, rt, ct, ct2, -1);
    tie(pf0);
    phase2<false>(p, shm, rt2, ct2, WS_HF + (unsigned)((rt * 12 + ct) * 64));
  }
  if (j >= 20) {
    const int rt = half * 4 + idx / 3;
    if (ty == 0) {
      phase1<1>(p, shm, rt, cg, ct2, rt * 4 + cg);
      tie(pf0);
      FINISH;
    }
    phase1<2>(p, shm, rt, 2 * cg + ty - 1, ct2, -1);
    tie(pf0);
    if (!has2) FINISH;
    phase2<true>(p, shm, rt2, ct2, 0u);
  }
  {
    const v4i rws = make_rsrc(p.ws, WS_END);
    const v4i rwd = make_rsrc(p.wd, DF_ * D_ * 2);
    const unsigned lds0 = (unsigned)(uintptr_t)shm;
    unsigned hb;
    {
      const v4u Z4 = {0u, 0u, 0u, 0u};
#pragma unroll
      for (int k = 0; k < 4; k++) { const unsigned o = (wid * 4 + k) * 1024 + lane * 16; ast16(Z4, rws, (x == 4 && o >= WS_GO) ? o : 0x7fffffffu, 0); }
    }
    ald4(hb, rws, WS_HB, 0);
    poison_store(rws, lane, wid);
    *(LAS unsigned*)(shm + T_CNT + (threadIdx.x & 15) * 4) = 0u;
    wait_vm<0>();
    bar();
    tie(hb);
    if (wid == 7) {
      if (x == 4) {
        set_flag(rws, WS_SIDE + blockIdx.x * 64, lane);
#pragma nounroll
        for (unsigned it = 0; it < SPIN_CAP; it++) {
          hb += 1u;
          ast4(hb, rws, lane == 0 ? WS_HB : 0x7fffffffu, 0);
          unsigned v[4];
#pragma unroll
          for (int k = 0; k < 4; k++) ald4(v[k], rws, lane * 64, WS_SIDE + k * 4096);
          wait_vm<0>();
#pragma unroll
          for (int k = 0; k < 4; k++) tie(v[k]);
          if (__all((v[0] & v[1] & v[2] & v[3]) != 0u)) break;
        }
        ast16(v4u{1u, 1u, 1u, 1u}, rws, WS_GO + lane * 16, 0);
      } else {
        spin_hb(rws, hb);
        set_flag(rws, WS_SIDE + blockIdx.x * 64, lane);
        spin_go(rws);
      }
    }
#pragma unroll
    for (int k = 0; k < 12; k++) wd_coop(rwd, lds0, wd_voff(ct2, lane), wid, k);
    wait_vm<0>();
    wait_lgkm0();
    bar();
  }
  tie(pf0);
  phase2<false>(p, shm, rt2, ct2, 0u);
}


extern "C" void kernel_launch(void* const* d_in, const int* in_sizes, int n_in, void* d_out, int out_size, void* d_ws,
                              size_t ws_size, hipStream_t stream) {
  Params p;
  p.x = (const __hip_bfloat16*)d_in[0];
  p.wqkv = (const __hip_bfloat16*)d_in[1];
  p.wo = (const __hip_bfloat16*)d_in[2];
  p.wg = (const __hip_bfloat16*)d_in[4];
  p.wu = (const __hip_bfloat16*)d_in[5];
  p.wd = (const __hip_bfloat16*)d_in[6];
  p.out = (__hip_bfloat16*)d_out;
  p.ws = (char*)d_ws;
  (void)hipFuncSetAttribute((const void*)fused, hipFuncAttributeMaxDynamicSharedMemorySize, 163840);
  fused<<<256, 512, 163840, stream>>>(p);
}
```
